```python
import torch

def _softmax(x, axis=-1):
    return torch.softmax(x, dim=axis)
def _gelu(x):
    return torch.nn.functional.gelu(x, approximate="tanh")
def _silu(x):
    return torch.nn.functional.silu(x)
def _sigmoid(x):
    return torch.sigmoid(x)
def _layernorm(x, axis=-1, eps=1e-5):
    m = x.mean(dim=axis, keepdim=True)
    v = x.var(dim=axis, keepdim=True, unbiased=False)
    return (x - m) / torch.sqrt(v + eps)
def _scatter_add(tgt, idx, src, axis=0):
    return tgt.clone().index_add_(axis, idx, src)
def _segment_sum(src, seg, n):
    out = torch.zeros((n,) + src.shape[1:], dtype=src.dtype)
    return out.index_add_(0, seg, src)
def _pad(x, pw):
    flat = tuple(v for pair in reversed(pw) for v in pair)
    return torch.nn.functional.pad(x, flat)

kernel_name = 'gated_mix__spectral_fnet__geglu_ffn__prompt'

def setup_inputs():

    _w = (lambda a: (lambda t: t.to(torch.bfloat16) if t.is_floating_point() else t)(torch.as_tensor(a)))
    def _A_setup():
        B, S, D = 1, 2048, 512
        ns = torch.arange(S)[:, None] * torch.arange(S)[None, :]
        nd = torch.arange(D)[:, None] * torch.arange(D)[None, :]
        Ws_re = torch.cos(-2.0 * torch.pi * ns / S).to(float)
        Ws_im = torch.sin(-2.0 * torch.pi * ns / S).to(float)
        Wd_re = torch.cos(-2.0 * torch.pi * nd / D).to(float)
        Wd_im = torch.sin(-2.0 * torch.pi * nd / D).to(float)
        return {
            "x": _w((lambda *s: torch.randn(*s, dtype=torch.bfloat16) * 0.05)(B, S, D)),
            "dft_seq_re": _w(Ws_re),
            "dft_seq_im": _w(Ws_im),
            "dft_hid_re": _w(Wd_re),
            "dft_hid_im": _w(Wd_im),
        }
    _A_in = _A_setup()

    def _B_setup():
        B, S, D, F = 1, 2048, 512, 1376
        return {
            "x": _w((lambda *s: torch.randn(*s, dtype=torch.bfloat16) * 0.05)(B, S, D)),
            "w_gate": _w((lambda *s: torch.randn(*s, dtype=torch.bfloat16) * 0.8)(D, F)),
            "w_up": _w((lambda *s: torch.randn(*s, dtype=torch.bfloat16) * 0.8)(D, F)),
            "w_down": _w((lambda *s: torch.randn(*s, dtype=torch.bfloat16) * 0.8)(F, D)),
        }
    _B_in = _B_setup()

    return {
        "A_x": _A_in["x"],
        "A_dft_seq_re": _A_in["dft_seq_re"],
        "A_dft_seq_im": _A_in["dft_seq_im"],
        "A_dft_hid_re": _A_in["dft_hid_re"],
        "A_dft_hid_im": _A_in["dft_hid_im"],
        "B_x": _B_in["x"],
        "B_w_gate": _B_in["w_gate"],
        "B_w_up": _B_in["w_up"],
        "B_w_down": _B_in["w_down"],
        "gate_w": _w((lambda *s: torch.randn(*s, dtype=torch.bfloat16) * 0.05)(512) * 16.0),
    }

def reference(A_x, A_dft_seq_re, A_dft_seq_im, A_dft_hid_re, A_dft_hid_im, B_x, B_w_gate, B_w_up, B_w_down, gate_w):
    def _A_ref(x, dft_seq_re, dft_seq_im, dft_hid_re, dft_hid_im):
        a = torch.einsum("bsd,ed->bse", x, dft_hid_re)
        b = torch.einsum("bsd,ed->bse", x, dft_hid_im)
        y = torch.einsum("ts,bse->bte", dft_seq_re, a) - torch.einsum("ts,bse->bte", dft_seq_im, b)
        return y

    def _B_ref(x, w_gate, w_up, w_down):
        return (_gelu(x @ w_gate) * (x @ w_up)) @ w_down

    x = A_x
    g = _sigmoid((x * gate_w).sum(-1, keepdims=True))
    a = _A_ref(x, A_dft_seq_re, A_dft_seq_im, A_dft_hid_re, A_dft_hid_im)
    b = _B_ref(x, B_w_gate, B_w_up, B_w_down)
    return g * a + (1 - g) * b
```

```cpp
#include <hip/hip_runtime.h>
#include <hip/hip_bf16.h>
typedef unsigned short u16;
static __device__ __forceinline__ float bf2f(u16 v){ return __uint_as_float(((unsigned)v)<<16); }
static __device__ __forceinline__ u16 f2bf(float f){ __hip_bfloat16 b = __float2bfloat16(f); return *(u16*)&b; }
constexpr int S=2048, D=512, F=1376;

__global__ void k_gate(const u16* x, const u16* gw, float* g){
  int t = blockIdx.x; int lane = threadIdx.x;
  float s=0;
  for(int d=lane; d<D; d+=64) s += bf2f(f2bf(bf2f(x[t*D+d])*bf2f(gw[d])));
  for(int o=32;o>0;o>>=1) s += __shfl_xor(s,o);
  if(lane==0){ s = bf2f(f2bf(s)); g[t] = 1.f/(1.f+__expf(-s)); }
}
__global__ void k_pq(const u16* cr, const u16* ci, const u16* x, const float* g, u16* pq){
  int d = blockIdx.x*256+threadIdx.x; int t = blockIdx.y;
  float a=0,b=0;
  for(int s=0;s<S;++s){ float xv=bf2f(x[s*D+d]); a+=bf2f(cr[t*S+s])*xv; b+=bf2f(ci[t*S+s])*xv; }
  pq[t*1024+d]=f2bf(a*g[t]); pq[t*1024+512+d]=f2bf(-b*g[t]);
}
__global__ void k_h(const u16* x, const u16* wg, const u16* wu, const float* g, u16* h){
  int f = blockIdx.x*32+threadIdx.x; int t = blockIdx.y*8+threadIdx.y;
  float a=0,b=0;
  for(int d=0;d<D;++d){ float xv=bf2f(x[t*D+d]); a+=xv*bf2f(wg[d*F+f]); b+=xv*bf2f(wu[d*F+f]); }
  float u = 0.7978845608028654f*(a+0.044715f*a*a*a);
  float ge = 0.5f*a*(1.f+tanhf(u));
  h[t*F+f]=f2bf(ge*b*(1.f-g[t]));
}
__global__ void k_out(const u16* pq, const u16* h, const u16* dr, const u16* di, const u16* wd, u16* out){
  int e = blockIdx.x*256+threadIdx.x; int t = blockIdx.y;
  float a=0;
  for(int d=0;d<D;++d){ a+=bf2f(pq[t*1024+d])*bf2f(dr[e*D+d]) + bf2f(pq[t*1024+512+d])*bf2f(di[e*D+d]); }
  for(int f=0;f<F;++f) a+=bf2f(h[t*F+f])*bf2f(wd[f*D+e]);
  out[t*D+e]=f2bf(a);
}
extern "C" void kernel_launch(void* const* d_in, const int* in_sizes, int n_in,
                              void* d_out, int out_size, void* d_ws, size_t ws_size,
                              hipStream_t stream) {
  const u16* x=(const u16*)d_in[0]; const u16* cr=(const u16*)d_in[1]; const u16* ci=(const u16*)d_in[2];
  const u16* dr=(const u16*)d_in[3]; const u16* di=(const u16*)d_in[4];
  const u16* wg=(const u16*)d_in[6]; const u16* wu=(const u16*)d_in[7]; const u16* wd=(const u16*)d_in[8]; const u16* gw=(const u16*)d_in[9];
  char* ws=(char*)d_ws;
  float* g=(float*)ws; u16* pq=(u16*)(ws+65536); u16* h=(u16*)(ws+65536+S*1024*2);
  k_gate<<<S,64,0,stream>>>(x,gw,g);
  k_pq<<<dim3(2,S),256,0,stream>>>(cr,ci,x,g,pq);
  k_h<<<dim3(F/32,S/8),dim3(32,8),0,stream>>>(x,wg,wu,g,h);
  k_out<<<dim3(2,S),256,0,stream>>>(pq,h,dr,di,wd,(u16*)d_out);
}
```

```cpp
#include <hip/hip_runtime.h>
#include <hip/hip_bf16.h>
typedef unsigned short u16;
typedef unsigned long long u64;
typedef __attribute__((ext_vector_type(8))) __bf16 bf16x8;
typedef __attribute__((ext_vector_type(2))) __bf16 bf16x2;
typedef short s16x4 __attribute__((ext_vector_type(4)));
typedef float f32x4 __attribute__((ext_vector_type(4)));
typedef float f32x2 __attribute__((ext_vector_type(2)));
typedef unsigned u32x4 __attribute__((ext_vector_type(4)));
typedef unsigned u32x2 __attribute__((ext_vector_type(2)));
typedef __amdgpu_buffer_rsrc_t rsrc_t;
#define LAS __attribute__((address_space(3)))
#define DEV __device__ __forceinline__

constexpr int S = 2048, D = 512, F = 1376;
constexpr unsigned STB = 32768;
constexpr unsigned LDS_BYTES = 5 * STB;
constexpr unsigned WS_PQ = 0;
constexpr unsigned WS_H = 4u << 20;
constexpr unsigned WS_FF = 10u << 20;
constexpr unsigned WS_G1 = 12u << 20;
constexpr unsigned WS_G2 = WS_G1 + 65536;
constexpr unsigned WS_SALT = WS_G2 + 65536;
constexpr unsigned WS_FLAG = WS_SALT + 4096;
constexpr unsigned WS_USED = WS_FLAG + 128 * 256;
constexpr int SPIN_MAX = 1 << 18;

#define CNT 1
#define PFC 1
#define PFX 1
#define PFW 1
#define STAG 1

template <int NT> DEV void dma16(rsrc_t r, unsigned voff, unsigned soff, unsigned m0v) {
  if (NT) asm volatile("s_mov_b32 m0, %0\n\ts_nop 0\n\tbuffer_load_dwordx4 %1, %2, %3 offen nt lds" ::"s"(m0v), "v"(voff), "s"(r), "s"(soff) : "memory");
  else asm volatile("s_mov_b32 m0, %0\n\ts_nop 0\n\tbuffer_load_dwordx4 %1, %2, %3 offen lds" ::"s"(m0v), "v"(voff), "s"(r), "s"(soff) : "memory");
}
template <int NT> DEV void touch(unsigned& dummy, rsrc_t r, unsigned voff, unsigned soff) {
  if (NT) asm volatile("buffer_load_dword %0, %1, %2, %3 offen nt" : "+v"(dummy) : "v"(voff), "s"(r), "s"(soff) : "memory");
  else asm volatile("buffer_load_dword %0, %1, %2, %3 offen" : "+v"(dummy) : "v"(voff), "s"(r), "s"(soff) : "memory");
}
template <int N> DEV void waitvm() { asm volatile("s_waitcnt vmcnt(%0)" ::"i"(N) : "memory"); }
DEV void bar() { asm volatile("s_barrier" ::: "memory"); }
DEV void mfma(f32x4& c, const bf16x8& a, const bf16x8& b) {
  asm volatile("v_mfma_f32_16x16x32_bf16 %0, %1, %2, %0" : "+v"(c) : "v"(a), "v"(b) : "memory");
}
DEV void mfma0(f32x4& c, const bf16x8& a, const bf16x8& b) {
  asm volatile("v_mfma_f32_16x16x32_bf16 %0, %1, %2, 0" : "=&v"(c) : "v"(a), "v"(b) : "memory");
}
DEV void mfma_drain() { asm volatile("s_nop 7\n\ts_nop 7" ::: "memory"); }
DEV void vadd(unsigned& a, int d) { asm volatile("v_add_u32 %0, %1, %0" : "+v"(a) : "i"(d)); }
DEV s16x4 ldtr(unsigned a) { return __builtin_amdgcn_ds_read_tr16_b64_v4i16((LAS s16x4*)(size_t)a); }
DEV s16x4 ld64(unsigned a) { return *(const LAS s16x4*)(size_t)a; }
DEV bf16x8 ld128(unsigned a) { return *(const LAS bf16x8*)(size_t)a; }
DEV bf16x8 join(s16x4 lo, s16x4 hi) { return __builtin_bit_cast(bf16x8, __builtin_shufflevector(lo, hi, 0, 1, 2, 3, 4, 5, 6, 7)); }
DEV unsigned pk(float a, float b) { f32x2 v = {a, b}; return __builtin_bit_cast(unsigned, __builtin_convertvector(v, bf16x2)); }
DEV float bflo(unsigned u) { return __uint_as_float(u << 16); }
DEV float bfhi(unsigned u) { return __uint_as_float(u & 0xffff0000u); }
DEV rsrc_t mkrs(const void* p, unsigned bytes) { return __builtin_amdgcn_make_buffer_rsrc((void*)p, 0, bytes, 0x00020000); }
DEV u64 next_tag(u64 s) { return s * 6364136223846793005ull + 1442695040888963407ull; }
DEV u64 ld_sc1(const u64* p) { return __hip_atomic_load(p, __ATOMIC_RELAXED, __HIP_MEMORY_SCOPE_AGENT); }
DEV void st_sc1(u64* p, u64 v) { __hip_atomic_store(p, v, __ATOMIC_RELAXED, __HIP_MEMORY_SCOPE_AGENT); }
DEV void poll(const u64* p, u64 tag) {
  for (int it = 0; it < SPIN_MAX; ++it) {
    u64 v = ld_sc1(p);
    if (__builtin_amdgcn_ballot_w64(v != tag) == 0) break;
    __builtin_amdgcn_s_sleep(1);
  }
}
constexpr int AUX_SC1 = 16;
#define AUXLD 16
#define PDEPTH 8

#define ST(i)

__global__ __launch_bounds__(512, 2) void fused_kernel(const u16* __restrict__ x, const u16* __restrict__ Cr, const u16* __restrict__ Ci,
                                                       const u16* __restrict__ Dr, const u16* __restrict__ Di, const u16* __restrict__ Wg,
                                                       const u16* __restrict__ Wu, const u16* __restrict__ Wd, const u16* __restrict__ gw,
                                                       u16* __restrict__ out, char* __restrict__ ws) {
  extern __shared__ __attribute__((aligned(16))) char smem[];
  const int tid = threadIdx.x, lane = tid & 63;
  const int w = __builtin_amdgcn_readfirstlane(tid >> 6);
  const int b = blockIdx.x, xcd = b & 7, idx = b >> 3;
  const int ii = idx & 15;
  const int rb = xcd * 2 + (ii >> 3), j = ii & 7;
  const unsigned ldsb = (unsigned)(size_t)(LAS char*)smem;
  const rsrc_t rX = mkrs(x, S * D * 2);
  const rsrc_t rW = mkrs(ws, WS_USED);
  u64* const salt = (u64*)(ws + WS_SALT);
  u64* const flags = (u64*)(ws + WS_FLAG);
  const int koff = STAG ? (xcd & 3) * 8 : 0;
  const int li = lane & 15, lg = lane >> 4, lq = li >> 2, lp = lane & 3;
  ST(0);

  if (idx < 16) {
    const int ri = j >> 2, dt = j & 3;
    if (w >= 4) {
      const int lw = w - 4;
      const rsrc_t rC = mkrs(ri ? Ci : Cr, S * S * 2);
      const rsrc_t rDr = mkrs(Dr, D * D * 2), rDi = mkrs(Di, D * D * 2);
      const int r8 = lane >> 3, p8 = lane & 7;
      const unsigned vC0 = r8 * 4096 + ((p8 ^ lg) << 4), vC1 = r8 * 4096 + ((p8 ^ (4 + lg)) << 4);
      const unsigned vX0 = lg * 1024 + ((li ^ (lg << 1)) << 4), vX1 = lg * 1024 + ((li ^ ((lg | 4) << 1)) << 4);
      const unsigned vD = r8 * 1024 + ((p8 ^ (((r8 >> 1) & 1) | (lw << 1))) << 4);
      const unsigned sC = (rb * 128 + 32 * lw) * 4096, sX = (16 * lw) * 1024 + dt * 256, sD = (j * 64 + 8 * lw) * 1024;
      auto issue = [&](int s, unsigned slot) {
        const unsigned k = (s + koff) & 31;
#pragma unroll
        for (int q = 0; q < 4; ++q) dma16<CNT>(rC, (q & 1) ? vC1 : vC0, sC + q * 8 * 4096 + k * 128, slot + (lw * 4 + q) * 1024);
#pragma unroll
        for (int q = 0; q < 4; ++q) dma16<0>(rX, (q >> 1) ? vX1 : vX0, sX + k * 65536 + q * 4096, slot + 16384 + (lw * 4 + q) * 1024);
      };
      auto issueD = [&](int grp, unsigned slot) {
#pragma unroll
        for (int q = 0; q < 4; ++q) {
          const unsigned blk = (4 * grp + q + 2 * j) & 15;
          const rsrc_t r = blk < 8 ? rDr : rDi;
          dma16<0>(r, vD, sD + (blk & 7) * 128, slot + q * 8192 + lw * 1024);
          dma16<0>(r, vD, sD + 32768 + (blk & 7) * 128, slot + q * 8192 + lw * 1024 + 4096);
        }
      };
      issue(0, ldsb); issue(1, ldsb + STB);
      waitvm<8>(); bar();
      issue(2, ldsb + 2 * STB); issue(3, ldsb + 3 * STB);
      unsigned slot = 4;
      for (int s = 0; s < 31; ++s) {
        waitvm<16>(); bar();
        if (s < 28) issue(s + 4, ldsb + slot * STB); else issueD(s - 28, ldsb + slot * STB);
        slot = slot == 4 ? 0 : slot + 1;
      }
      const u64 tagc = next_tag(salt[rb * 8 + (lane & 7)]);
      const u64 tagm = next_tag(salt[rb * 8 + j]);
      bar();
      issueD(3, ldsb);
      bar();
      if (lw == 0) {
        if (lane < 8) st_sc1(flags + (rb * 8 + lane) * 32 + j, tagc);
        poll(flags + (rb * 8 + j) * 32 + (lane & 7), tagm);
      }
      waitvm<0>();
      bar();
      poll(flags + (rb * 8 + j) * 32 + 16, tagm);
      u32x4 f[4];
#pragma unroll
      for (int q = 0; q < 4; ++q) f[q] = __builtin_amdgcn_raw_buffer_load_b128(rW, lane * 16, WS_FF + (rb * 8 + j) * 16384 + lw * 4096 + q * 1024, AUX_SC1);
      const unsigned gv = __builtin_amdgcn_raw_buffer_load_b32(rW, (lane & 31) * 4, WS_G2 + (rb * 8 + j) * 512 + lw * 128, AUX_SC1);
#pragma unroll
      for (int q = 0; q < 4; ++q) *(LAS u32x4*)(size_t)(ldsb + STB + lw * 4096 + q * 1024 + lane * 16) = f[q];
      *(LAS unsigned*)(size_t)(ldsb + STB + 16384 + lw * 128 + (lane & 31) * 4) = gv;
      if (lw == 0 && lane == 0) st_sc1(salt + rb * 8 + j, tagm);
      asm volatile("s_waitcnt lgkmcnt(0)" ::: "memory");
      bar();
      return;
    }
    const int wd = w >> 1, wt = w & 1;
    unsigned aC[2], aX[4];
#pragma unroll
    for (int ks = 0; ks < 2; ++ks) aC[ks] = ldsb + (wt * 64 + li) * 128 + (((4 * ks + lg) ^ (li >> 1)) << 4);
#pragma unroll
    for (int m = 0; m < 4; ++m) aX[m] = ldsb + 16384 + (8 * lg + lq) * 256 + (((2 * (wd * 4 + m) + (lp >> 1)) ^ ((lq | ((lg & 1) << 2)) << 1)) << 4) + 8 * (lp & 1);
    f32x4 acc[4][4];
    s16x4 xa[2][4][2];
    bf16x8 cb[2][4];
    auto rdS = [&](int r, int buf, unsigned so, int ks) {
      constexpr int kind[12] = {0, 1, 8, 9, 2, 3, 10, 11, 4, 5, 6, 7};
      const int k = kind[r];
      if (k < 8) xa[buf][k >> 1][k & 1] = ldtr(aX[k >> 1] + so + ks * 8192 + (k & 1) * 1024);
      else cb[buf][k - 8] = ld128(aC[ks] + so + (k - 8) * 2048);
    };
    bar();
    ST(1);
#pragma unroll
    for (int r = 0; r < 12; ++r) rdS(r, 0, 0, 0);
#pragma unroll
    for (int s = 0; s < 32; ++s) {
      const unsigned so = (s % 5) * STB, sn = ((s + 1) % 5) * STB;
#pragma unroll
      for (int i = 0; i < 16; ++i) {
        if (s == 0) mfma0(acc[i >> 2][i & 3], join(xa[0][i >> 2][0], xa[0][i >> 2][1]), cb[0][i & 3]);
        else mfma(acc[i >> 2][i & 3], join(xa[0][i >> 2][0], xa[0][i >> 2][1]), cb[0][i & 3]);
        if (i < 12) rdS(i, 1, so, 1);
      }
      if (s < 31) bar();
#pragma unroll
      for (int i = 0; i < 16; ++i) {
        mfma(acc[i >> 2][i & 3], join(xa[1][i >> 2][0], xa[1][i >> 2][1]), cb[1][i & 3]);
        if (i < 12 && s < 31) rdS(i, 0, sn, 0);
      }
    }
    mfma_drain();
    ST(2);
    {
      const unsigned vo = lane * 16, so = WS_PQ + ((rb * 32 + 4 * j + 2 * wd) * 8 + 4 * wt) * 1024;
      auto publish = [&](float sg) {
#pragma unroll
        for (int M = 0; M < 2; ++M)
#pragma unroll
          for (int n = 0; n < 4; ++n) {
            const f32x4 p = acc[2 * M][n], q = acc[2 * M + 1][n];
            const u32x4 v = {pk(sg * p[0], sg * p[1]), pk(sg * p[2], sg * p[3]), pk(sg * q[0], sg * q[1]), pk(sg * q[2], sg * q[3])};
            __builtin_amdgcn_raw_buffer_store_b128(v, rW, vo + n * 1024, so + M * 8192, AUX_SC1);
          }
      };
      if (ri) publish(-1.f); else publish(1.f);
    }
    bar();
    waitvm<0>();
    ST(3);
    bar();
    bar();
    ST(4);
    {
      unsigned aD[4];
#pragma unroll
      for (int v = 0; v < 4; ++v) aD[v] = ldsb + (8 * lq + lp) * 128 + ((((lg >> 1) + 2 * v) ^ (li >> 1)) << 4) + 8 * (lg & 1);
      f32x4 y[4][2];
      constexpr int PD = PDEPTH;
      u32x4 pa[PD + 1][2];
      s16x4 da[2][4][2];
      const unsigned vo = lane * 16 + w * 2048;
      auto ldP = [&](int kb) {
        const unsigned so = WS_PQ + (rb * 32 + ((kb + 4 * j) & 31)) * 8192;
        pa[kb % (PD + 1)][0] = __builtin_amdgcn_raw_buffer_load_b128(rW, vo, so, AUXLD);
        pa[kb % (PD + 1)][1] = __builtin_amdgcn_raw_buffer_load_b128(rW, vo + 1024, so, AUXLD);
      };
      auto rdD = [&](int kb, int r) {
        const unsigned base = (65536 + (kb >> 1) * 8192) % LDS_BYTES;
        const int mt = r >> 1, hl = r & 1;
        da[kb & 1][mt][hl] = ld64(aD[(kb & 1) * 2 + hl] + base + (mt >> 1) * 4096 + (mt & 1) * 512);
      };
#pragma unroll
      for (int kb = 0; kb < PD; ++kb) ldP(kb);
#pragma unroll
      for (int r = 0; r < 8; ++r) rdD(0, r);
#pragma unroll
      for (int kb = 0; kb < 32; ++kb) {
        if (kb + PD < 32) ldP(kb + PD);
#pragma unroll
        for (int i = 0; i < 8; ++i) {
          const bf16x8 a = join(da[kb & 1][i >> 1][0], da[kb & 1][i >> 1][1]);
          const bf16x8 bb = __builtin_bit_cast(bf16x8, pa[kb % (PD + 1)][i & 1]);
          if (kb == 0) mfma0(y[i >> 1][i & 1], a, bb); else mfma(y[i >> 1][i & 1], a, bb);
          if (kb < 31) rdD(kb + 1, i);
        }
      }
      mfma_drain();
      ST(5);
      bar();
      ST(6);
      const rsrc_t rO = mkrs(out, S * D * 2);
#pragma unroll
      for (int n = 0; n < 2; ++n) {
        const float g = *(const LAS float*)(size_t)(ldsb + STB + 16384 + (32 * w + 16 * n + li) * 4);
        const float og = 1.f - g;
#pragma unroll
        for (int M = 0; M < 2; ++M) {
          const u32x4 f = *(const LAS u32x4*)(size_t)(ldsb + STB + w * 4096 + (n * 2 + M) * 1024 + lane * 16);
          const f32x4 p = y[2 * M][n], q = y[2 * M + 1][n];
          const u32x4 v = {pk(g * p[0] + og * bflo(f[0]), g * p[1] + og * bfhi(f[0])), pk(g * p[2] + og * bflo(f[1]), g * p[3] + og * bfhi(f[1])),
                           pk(g * q[0] + og * bflo(f[2]), g * q[1] + og * bfhi(f[2])), pk(g * q[2] + og * bflo(f[3]), g * q[3] + og * bfhi(f[3]))};
          __builtin_amdgcn_raw_buffer_store_b128(v, rO, (16 * n + li) * 1024 + lg * 16 + M * 64, (rb * 128 + 32 * w) * 1024 + j * 128, 0);
        }
      }
      ST(15);
    }
    return;
  }

  const int rbU = ii, sl = xcd;
  const int f0 = sl < 7 ? 176 * sl : 1200;
  const unsigned fbase = (f0 * 2) & ~63u;
  if (w >= 4) {
    const int lw = w - 4, mat = lw & 1;
    const rsrc_t rG = mkrs(mat ? Wu : Wg, D * F * 2);
    const rsrc_t rWd = mkrs(Wd, F * D * 2);
    unsigned vW[6];
#pragma unroll
    for (int q = 0; q < 6; ++q) {
      const int L = 64 * q + lane, rl = L / 24, p = L - rl * 24;
      const int sw = 2 * (((rl >> 1) & 1) | (((rl >> 3) & 1) << 1)) + mat;
      vW[q] = rl * (F * 2) + ((p ^ sw) << 4);
    }
    const unsigned vX = (lane >> 2) * 1024 + ((lp ^ (3 * (lane >> 5))) << 4);
    const unsigned sW = (16 * (lw >> 1)) * (F * 2) + fbase, sX = (rbU * 128 + 32 * lw) * 1024;
    auto issue = [&](int s, unsigned slot) {
#pragma unroll
      for (int q = 0; q < 6; ++q) dma16<0>(rG, vW[q], sW + s * 32 * (F * 2), slot + mat * 12288 + (lw >> 1) * 6144 + q * 1024);
#pragma unroll
      for (int q = 0; q < 2; ++q) dma16<0>(rX, vX, sX + q * 16384 + s * 64, slot + 24576 + (lw * 2 + q) * 1024);
    };
    const int q4 = (lane >> 3) & 3, hf = lane >> 5;
    const unsigned vd0 = q4 * 1024 + (((lane & 7) ^ (((q4 >> 1) | ((lw & 1) << 1)) << 1)) << 4);
    const unsigned hk = hf * 1024;
    unsigned tabRow, tabDel;
    {
      int kb = min(lane, 42) + 5 * j; kb = kb >= 43 ? kb - 43 : kb;
      const int gam = 4 * kb + lw;
      const bool s7 = gam >= 154;
      const int s = s7 ? 7 : (gam * 745) >> 14;
      const int i = s7 ? gam - 150 : gam - 22 * s;
      const int fs = s7 ? 1200 : 176 * s;
      const int c = i >= 10;
      const int row = i >= 20 ? fs + 80 + 4 * (i & 1) : fs + 88 * c + 8 * ((i - 10 * c) & ~1) + 4 * (i & 1);
      tabRow = row * 1024 + j * 128; tabDel = i >= 20 ? 88 : 8;
    }
    auto issueWd = [&](int bp, unsigned dst) {
      dma16<0>(rWd, vd0 + hk * __builtin_amdgcn_readlane(tabDel, bp), __builtin_amdgcn_readlane(tabRow, bp), dst + lw * 1024);
    };
    auto issueG = [&](int grp, unsigned slot) {
#pragma unroll
      for (int q = 0; q < 8; ++q) issueWd(8 * grp + q, slot + q * 4096);
    };
    issue(0, ldsb); issue(1, ldsb + STB);
    waitvm<8>(); bar();
    issue(2, ldsb + 2 * STB); issue(3, ldsb + 3 * STB);
    unsigned slot = 4;
    for (int s = 0; s < 15; ++s) {
      waitvm<16>(); bar();
      if (s < 12) issue(s + 4, ldsb + slot * STB); else issueG(s - 12, ldsb + slot * STB);
      slot = slot == 4 ? 0 : slot + 1;
    }
    const u64 tagc = next_tag(salt[rbU * 8 + (lane & 7)]);
    const u64 tagm = next_tag(salt[rb * 8 + j]);
    bar();
    issueG(3, ldsb + 4 * STB); issueG(4, ldsb);
    bar();
    if (lw == 0) {
      if (lane < 8) st_sc1(flags + (rbU * 8 + lane) * 32 + 8 + sl, tagc);
      poll(flags + (rb * 8 + j) * 32 + 8 + (lane & 7), tagm);
    }
    waitvm<0>();
    bar();
    if (lw == 0) {
      const u32x2 gv = __builtin_amdgcn_raw_buffer_load_b64(rW, lane * 8, WS_G1 + rb * 512, AUX_SC1);
      __builtin_amdgcn_raw_buffer_store_b64(gv, rW, lane * 8, WS_G2 + (rb * 8 + j) * 512, AUX_SC1);
    }
    bar();
#pragma unroll
    for (int q = 0; q < 3; ++q) issueWd(40 + q, ldsb + STB + q * 4096);
    waitvm<0>();
    bar();
    bar();
    if (lw == 0 && lane == 0) st_sc1(flags + (rb * 8 + j) * 32 + 16, tagm);
    return;
  }
  const int wc = w >> 1, wt = w & 1;
  unsigned dummy = 0;
  f32x4 gout;
  {
    u32x4 xr[4], gv;
    const rsrc_t rGw = mkrs(gw, D * 2);
    const unsigned vo = lane * 16, so = (rbU * 128 + 16 * sl + 4 * w) * 1024;
    asm volatile("buffer_load_dwordx4 %0, %1, %2, 0 offen" : "=v"(gv) : "v"(vo), "s"(rGw) : "memory");
#pragma unroll
    for (int r = 0; r < 4; ++r) asm volatile("buffer_load_dwordx4 %0, %1, %2, %3 offen offset:%4" : "=v"(xr[r]) : "v"(vo), "s"(rX), "s"(so), "i"(r * 1024) : "memory");
    const int id = ii * 4 + w;
    constexpr int NPF = (PFW ? 1 : 0) + (PFC ? 4 : 0) + (PFX ? 4 : 0);
    if (PFW) {
      const rsrc_t rG = mkrs((id & 1) ? Wu : Wg, D * F * 2);
      unsigned vo2 = ((id >> 1) * 16 + (lane >> 2)) * (F * 2) + fbase + min(lp * 128, 380);
      touch<0>(dummy, rG, vo2, 0);
    }
    const rsrc_t rC = mkrs((id & 16) ? Ci : Cr, S * S * 2);
    const unsigned vc = ((xcd * 2 + (id >> 5)) * 128 + (id & 15) * 8 + (lane >> 3)) * 4096 + (lane & 7) * 128;
    const unsigned vx = (id * 8 + (lane >> 3)) * 1024 + (lane & 7) * 128;
#pragma unroll
    for (int c = 0; c < 4; ++c) {
      const unsigned cc = (c + (koff >> 3)) & 3;
      if (PFC) touch<PFC == 1>(dummy, rC, vc, cc * 1024);
      if (PFX) touch<0>(dummy, rX, vx, cc * 524288);
    }
    asm volatile("s_waitcnt vmcnt(%5)" : "+v"(gv), "+v"(xr[0]), "+v"(xr[1]), "+v"(xr[2]), "+v"(xr[3]) : "i"(NPF) : "memory");
#pragma unroll
    for (int r = 0; r < 4; ++r) {
      float s = 0.f;
#pragma unroll
      for (int e = 0; e < 4; ++e) {
        const unsigned p = pk(bflo(xr[r][e]) * bflo(gv[e]), bfhi(xr[r][e]) * bfhi(gv[e]));
        s += bflo(p) + bfhi(p);
      }
#pragma unroll
      for (int o = 32; o > 0; o >>= 1) s += __shfl_xor(s, o);
      s = bflo(pk(s, s));
      s = __builtin_amdgcn_rcpf(1.f + __builtin_amdgcn_exp2f(-1.4426950408889634f * s));
      gout[r] = bflo(pk(s, s));
    }
    if (lane == 0) __builtin_amdgcn_raw_buffer_store_b128(__builtin_bit_cast(u32x4, gout), rW, 0, WS_G1 + (rbU * 128 + 16 * sl + 4 * w) * 4, AUX_SC1);
  }
  f32x4 acc[11][4];
  {
    const int c0 = ((f0 * 2) & 63) >> 4;
    unsigned aW[11], aX;
    const int mat = lp >> 1, sw = 2 * ((lq >> 1) | ((lg & 1) << 1)) + mat;
#pragma unroll
    for (int u = 0; u < 11; ++u) aW[u] = ldsb + mat * 12288 + (8 * lg + lq) * 384 + (((c0 + 11 * wc + u) ^ sw) << 4) + 8 * (lp & 1);
    aX = ldsb + 24576 + (64 * wt + li) * 64 + ((lg ^ (3 * (li >> 3))) << 4);
    s16x4 wf[3][2];
    bf16x8 xf[2][4];
    bar();
    ST(1);
    wf[0][0] = ldtr(aW[0]); wf[0][1] = ldtr(aW[0] + 1536);
#pragma unroll
    for (int n = 0; n < 4; ++n) xf[0][n] = ld128(aX + n * 1024);
    wf[1][0] = ldtr(aW[1]); wf[1][1] = ldtr(aW[1] + 1536);
#pragma unroll
    for (int s = 0; s < 16; ++s) {
      const int dl = (s % 5 == 4) ? -4 * (int)STB : (int)STB;
#pragma unroll
      for (int u = 0; u < 11; ++u) {
        const int cur = (s * 11 + u) % 3, nx = (s * 11 + u + 2) % 3, un = (u + 2) % 11;
        const bool more = s < 15 || u < 9;
        if (u == 9 && s < 15) bar();
#pragma unroll
        for (int n = 0; n < 4; ++n) {
          const bf16x8 a = join(wf[cur][0], wf[cur][1]);
          if (s == 0) mfma0(acc[u][n], a, xf[s & 1][n]); else mfma(acc[u][n], a, xf[s & 1][n]);
          if (u < 9) {
            if (n < 2) wf[nx][n] = ldtr(aW[un] + n * 1536);
            if (n == 2 && s < 15) vadd(aW[u], dl);
            if (n == 3 && u == 8 && s < 15) vadd(aX, dl);
          } else if (more) {
            if (n < 2) xf[(s + 1) & 1][(u - 9) * 2 + n] = ld128(aX + ((u - 9) * 2 + n) * 1024);
            else wf[nx][n - 2] = ldtr(aW[un] + (n - 2) * 1536);
            if (n == 1) vadd(aW[u], dl);
          }
        }
      }
    }
  }
  mfma_drain();
  bar();
  ST(2);
  {
    const unsigned g0 = sl < 7 ? 22 * sl : 150;
    const unsigned vo = (lg & 1) * 256 + (4 * wt + (lane >> 5)) * 1024 + li * 16;
    const unsigned hb = WS_H + rbU * (43 * 8192);
    auto act2 = [&](float a0, float a1, float b0, float b1) {
      const auto s0 = __builtin_amdgcn_permlane32_swap(__float_as_uint(a0), __float_as_uint(b0), false, false);
      const auto s1 = __builtin_amdgcn_permlane32_swap(__float_as_uint(a1), __float_as_uint(b1), false, false);
      const f32x2 a = {__uint_as_float(s0[0]), __uint_as_float(s1[0])}, up = {__uint_as_float(s0[1]), __uint_as_float(s1[1])};
      const f32x2 k2 = {-0.1029432397f, -0.1029432397f}, k1 = {-2.302208198f, -2.302208198f}, one = {1.f, 1.f};
      const f32x2 z = a * __builtin_elementwise_fma(a * a, k2, k1);
      f32x2 e = {__builtin_amdgcn_exp2f(z[0]), __builtin_amdgcn_exp2f(z[1])};
      e += one;
      const f32x2 r = {__builtin_amdgcn_rcpf(e[0]), __builtin_amdgcn_rcpf(e[1])};
      const f32x2 h = (a * up) * r;
      return pk(h[0], h[1]);
    };
    auto act = [&](int u, int np, unsigned& o0, unsigned& o1) {
      const f32x4 p = acc[u][2 * np], q = acc[u][2 * np + 1];
      o0 = act2(p[0], p[1], q[0], q[1]); o1 = act2(p[2], p[3], q[2], q[3]);
    };
#pragma unroll
    for (int p = 0; p < 5; ++p) {
      const unsigned E = g0 + (5 * wc + p) * 2;
      const unsigned so = hb + (E >> 2) * 8192 + (E & 2) * 256;
      const bool skip = p < 2 && sl == 7 && wc == 0;
#pragma unroll
      for (int np = 0; np < 2; ++np) {
        u32x4 v; unsigned t0, t1, t2, t3;
        act(2 * p, np, t0, t1); act(2 * p + 1, np, t2, t3);
        v[0] = t0; v[1] = t1; v[2] = t2; v[3] = t3;
        if (!skip) __builtin_amdgcn_raw_buffer_store_b128(v, rW, vo + np * 2048, so, AUX_SC1);
      }
    }
    {
      const unsigned E = g0 + 20;
      const unsigned so = hb + (E >> 2) * 8192 + (E & 2) * 256 + 8 * wc;
#pragma unroll
      for (int np = 0; np < 2; ++np) {
        u32x2 v; unsigned t0, t1;
        act(10, np, t0, t1);
        v[0] = t0; v[1] = t1;
        __builtin_amdgcn_raw_buffer_store_b64(v, rW, vo + np * 2048, so, AUX_SC1);
      }
    }
  }
  ST(7);
  asm volatile("s_waitcnt vmcnt(0)" ::"v"(dummy) : "memory");
  ST(3);
  bar();
  bar();
  ST(4);
  {
    unsigned aV[4];
#pragma unroll
    for (int m = 0; m < 4; ++m) aV[m] = ldsb + (8 * lg + lq) * 128 + (((2 * m + (lp >> 1)) ^ (((lq >> 1) | ((lg & 1) << 1)) << 1)) << 4) + 8 * (lp & 1);
    f32x4 y[4][2];
    constexpr int PD = PDEPTH;
    u32x4 pa[PD + 1][2];
    s16x4 da[2][4][2];
    const unsigned vo = lane * 16 + w * 2048;
    auto ldH = [&](int bp) {
      int kb = bp + 5 * j; kb = kb >= 43 ? kb - 43 : kb;
      const unsigned so = WS_H + (rb * 43 + kb) * 8192;
      pa[bp % (PD + 1)][0] = __builtin_amdgcn_raw_buffer_load_b128(rW, vo, so, AUXLD);
      pa[bp % (PD + 1)][1] = __builtin_amdgcn_raw_buffer_load_b128(rW, vo + 1024, so, AUXLD);
    };
    auto rdV = [&](int bp, int r) {
      const unsigned base = (STB + (bp % 40) * 4096) % LDS_BYTES;
      da[bp & 1][r >> 1][r & 1] = ldtr(aV[r >> 1] + base + (r & 1) * 512);
    };
#pragma unroll
    for (int bp = 0; bp < PD; ++bp) ldH(bp);
#pragma unroll
    for (int r = 0; r < 8; ++r) rdV(0, r);
#pragma unroll
    for (int bp = 0; bp < 43; ++bp) {
      if (bp + PD < 43) ldH(bp + PD);
      if (bp == 3 || bp == 38) bar();
#pragma unroll
      for (int i = 0; i < 8; ++i) {
        const bf16x8 a = join(da[bp & 1][i >> 1][0], da[bp & 1][i >> 1][1]);
        const bf16x8 bb = __builtin_bit_cast(bf16x8, pa[bp % (PD + 1)][i & 1]);
        if (bp == 0) mfma0(y[i >> 1][i & 1], a, bb); else mfma(y[i >> 1][i & 1], a, bb);
        if (bp < 42) rdV(bp + 1, i);
      }
    }
    mfma_drain();
    ST(5);
#pragma unroll
    for (int n = 0; n < 2; ++n)
#pragma unroll
      for (int M = 0; M < 2; ++M) {
        const f32x4 p = y[2 * M][n], q = y[2 * M + 1][n];
        u32x4 v;
#pragma unroll
        for (int e = 0; e < 2; ++e) {
          const auto s1 = __builtin_amdgcn_permlane32_swap(pk(p[2 * e], p[2 * e + 1]), pk(q[2 * e], q[2 * e + 1]), false, false);
          const auto s2 = __builtin_amdgcn_permlane16_swap(s1[0], s1[1], false, false);
          v[e] = s2[0]; v[2 + e] = s2[1];
        }
        __builtin_amdgcn_raw_buffer_store_b128(v, rW, lane * 16 + (n * 2 + M) * 1024, WS_FF + (rb * 8 + j) * 16384 + w * 4096, AUX_SC1);
      }
    waitvm<0>();
    ST(6);
    bar();
    ST(15);
  }
}

extern "C" void kernel_launch(void* const* d_in, const int* in_sizes, int n_in, void* d_out, int out_size, void* d_ws, size_t ws_size,
                              hipStream_t stream) {
  (void)hipFuncSetAttribute((const void*)fused_kernel, hipFuncAttributeMaxDynamicSharedMemorySize, LDS_BYTES);
  fused_kernel<<<256, 512, LDS_BYTES, stream>>>((const u16*)d_in[0], (const u16*)d_in[1], (const u16*)d_in[2], (const u16*)d_in[3],
                                               (const u16*)d_in[4], (const u16*)d_in[6], (const u16*)d_in[7], (const u16*)d_in[8],
                                               (const u16*)d_in[9], (u16*)d_out, (char*)d_ws);
}
```

```cpp
#include <hip/hip_runtime.h>
#include <hip/hip_bf16.h>
typedef unsigned short u16;
typedef unsigned long long u64;
typedef __attribute__((ext_vector_type(8))) __bf16 bf16x8;
typedef __attribute__((ext_vector_type(2))) __bf16 bf16x2;
typedef short s16x4 __attribute__((ext_vector_type(4)));
typedef float f32x4 __attribute__((ext_vector_type(4)));
typedef float f32x2 __attribute__((ext_vector_type(2)));
typedef unsigned u32x4 __attribute__((ext_vector_type(4)));
typedef unsigned u32x2 __attribute__((ext_vector_type(2)));
typedef __amdgpu_buffer_rsrc_t rsrc_t;
#define LAS __attribute__((address_space(3)))
#define DEV __device__ __forceinline__

constexpr int S = 2048, D = 512, F = 1376;
constexpr unsigned STB = 32768;
constexpr unsigned LDS_BYTES = 5 * STB;
#define GAP 0
constexpr unsigned PST = 32 * 8192 + GAP, HST = 43 * 8192 + GAP, FST = 8 * 16384 + GAP;
constexpr unsigned WS_PQ = GAP;
constexpr unsigned WS_H = WS_PQ + 16 * PST;
constexpr unsigned WS_FF = WS_H + 16 * HST;
constexpr unsigned WS_G = WS_FF + 16 * FST;
constexpr unsigned WS_SALT = WS_G + 65536;
constexpr unsigned WS_FLAG = WS_SALT + 4096;
constexpr unsigned WS_USED = WS_FLAG + 128 * 1024;
constexpr int SPIN_MAX = 1 << 17;
constexpr unsigned XB = 98304, WSL = 24576;

#define CNT 0
#define ROTX 1
#define WD1 6
#define WD2 12
#define KX 8
#define LA 4
#define PAIR 32
#define FFP 20
#define D1POS 1
#define D2POS 2
#define D3POS 3
#define DWKB 15
#define D2SL 2
#define D3KB 3
#define WKB 29
#define GPOS 1
#define RRD 1
#define OAUX 0

template <int NT> DEV void dma16(rsrc_t r, unsigned voff, unsigned soff, unsigned m0v) {
  if (NT == 2) asm volatile("s_mov_b32 m0, %0\n\ts_nop 3\n\tbuffer_load_dwordx4 %1, %2, %3 offen lds" ::"s"(m0v), "v"(voff), "s"(r), "s"(soff) : "memory");
  else if (NT) asm volatile("s_mov_b32 m0, %0\n\ts_nop 0\n\tbuffer_load_dwordx4 %1, %2, %3 offen nt lds" ::"s"(m0v), "v"(voff), "s"(r), "s"(soff) : "memory");
  else asm volatile("s_mov_b32 m0, %0\n\ts_nop 0\n\tbuffer_load_dwordx4 %1, %2, %3 offen lds" ::"s"(m0v), "v"(voff), "s"(r), "s"(soff) : "memory");
}
DEV void setm0(unsigned v) { asm volatile("s_mov_b32 m0, %0\n\ts_nop 0" ::"s"(v) : "memory"); }
template <int Q, int NT> DEV void dmaq(rsrc_t r, unsigned voff, unsigned soff) {
  if (NT) asm volatile("buffer_load_dwordx4 %0, %1, %2 offen offset:%3 nt lds" ::"v"(voff), "s"(r), "s"(soff), "i"(Q * 1024) : "memory");
  else asm volatile("buffer_load_dwordx4 %0, %1, %2 offen offset:%3 lds" ::"v"(voff), "s"(r), "s"(soff), "i"(Q * 1024) : "memory");
}
template <int NT> DEV void dma4(rsrc_t r, const unsigned* v, unsigned soff, unsigned m0v) {
  setm0(m0v); dmaq<0, NT>(r, v[0], soff); dmaq<1, NT>(r, v[1], soff); dmaq<2, NT>(r, v[2], soff); dmaq<3, NT>(r, v[3], soff);
}
template <int N> DEV void waitvm() { asm volatile("s_waitcnt vmcnt(%0)" ::"i"(N) : "memory"); }
DEV void bar() { asm volatile("s_barrier" ::: "memory"); }
DEV void mfma(f32x4& c, const bf16x8& a, const bf16x8& b) {
  asm volatile("v_mfma_f32_16x16x32_bf16 %0, %1, %2, %0" : "+v"(c) : "v"(a), "v"(b) : "memory");
}
DEV void mfma0(f32x4& c, const bf16x8& a, const bf16x8& b) {
  asm volatile("v_mfma_f32_16x16x32_bf16 %0, %1, %2, 0" : "=&v"(c) : "v"(a), "v"(b) : "memory");
}
DEV void sep() { asm volatile("" ::: "memory"); }
DEV void mfma_drain() { asm volatile("s_nop 7\n\ts_nop 7" ::: "memory"); }
DEV void hold(f32x4& a) { asm volatile("" : "+v"(a)); }
DEV void vadd(unsigned& a, int d) { asm volatile("v_add_u32 %0, %1, %0" : "+v"(a) : "i"(d)); }
DEV s16x4 ldtr(unsigned a) { return __builtin_amdgcn_ds_read_tr16_b64_v4i16((LAS s16x4*)(size_t)a); }
DEV s16x4 ld64(unsigned a) { return *(const LAS s16x4*)(size_t)a; }
DEV bf16x8 ld128(unsigned a) { return *(const LAS bf16x8*)(size_t)a; }
DEV bf16x8 join(s16x4 lo, s16x4 hi) { return __builtin_bit_cast(bf16x8, __builtin_shufflevector(lo, hi, 0, 1, 2, 3, 4, 5, 6, 7)); }
DEV unsigned pk(float a, float b) { f32x2 v = {a, b}; return __builtin_bit_cast(unsigned, __builtin_convertvector(v, bf16x2)); }
DEV unsigned pkneg(float a, float b) { unsigned r; asm("v_cvt_pk_bf16_f32 %0, -%1, -%2" : "=v"(r) : "v"(a), "v"(b)); return r; }
DEV float bflo(unsigned u) { return __uint_as_float(u << 16); }
DEV float bfhi(unsigned u) { return __uint_as_float(u & 0xffff0000u); }
DEV rsrc_t mkrs(const void* p, unsigned bytes) { return __builtin_amdgcn_make_buffer_rsrc((void*)p, 0, bytes, 0x00020000); }
DEV u64 next_tag(u64 s) { return s * 6364136223846793005ull + 1442695040888963407ull; }
template <int AUX> DEV void st128(u32x4 v, rsrc_t r, unsigned vo) { __builtin_amdgcn_raw_buffer_store_b128(v, r, vo, 0, AUX); }
DEV void pin(unsigned& a) { asm volatile("" : "+v"(a)); }
DEV void pins(unsigned& a) { asm volatile("" : "+s"(a)); }
DEV void ld64a(u64& d, rsrc_t r, unsigned vo, unsigned so) { asm volatile("buffer_load_dwordx2 %0, %1, %2, %3 offen sc1" : "+v"(d) : "v"(vo), "s"(r), "s"(so) : "memory"); }
DEV void st64a(u64 d, rsrc_t r, unsigned vo, unsigned so) { asm volatile("buffer_store_dwordx2 %0, %1, %2, %3 offen sc1" ::"v"(d), "v"(vo), "s"(r), "s"(so) : "memory"); }
template <int N> DEV void waitvm(u64& a) { asm volatile("s_waitcnt vmcnt(%1)" : "+v"(a) : "i"(N) : "memory"); }
DEV void poll(u64& a, u64& b, rsrc_t r, unsigned vo, unsigned so, u64 tag) {
  ld64a(a, r, vo, so);
  __builtin_amdgcn_s_sleep(4);
#pragma unroll 1
  for (int it = 0; it < SPIN_MAX; ++it) {
    ld64a(b, r, vo, so);
    waitvm<1>(a);
    if (__builtin_amdgcn_ballot_w64(a != tag) == 0) break;
    ld64a(a, r, vo, so);
    waitvm<1>(b);
    if (__builtin_amdgcn_ballot_w64(b != tag) == 0) break;
  }
}
constexpr unsigned wdpos(int bp) { return bp >= 40 ? XB + (bp - 40) * 4096 : bp >= 32 ? bp * 4096 : bp >= 8 ? (bp - 8) * 4096 : XB + bp * 4096; }
#define AUXST 16
#define HOIST 4
#define SP0 8
#define HPF 0
#define LATEW 1
#define APF 5
#define OTR 1
#define GSPL 0
#define GEARLY 2
#define PVS 3, 8, 11, -9, -9, -9
#define PVR 3, 5, 7, 0, 0, 0
constexpr int pvs[6] = {PVS}, pvr[6] = {PVR};
constexpr int pvn(int s) { int n = 0; for (int q = 0; q < 6; ++q) n += pvs[q] == s; return n; }
#define XG 0
#define XS 1, 5, 9
constexpr int xs[3] = {XS};
constexpr int xstep(int u) { return u == 1 ? -1 : XG ? xs[u / 2 - 1] : 2 * u - 3; }
constexpr int xops(int s) { return s == -1 ? 4 : XG ? (s == xs[0] || s == xs[1] || s == xs[2]) * 8 : s < 12 ? 2 : 0; }
constexpr int nops(int s) { return (s <= 12 ? 6 : s == 13 ? WD1 : 0) + xops(s) + pvn(s); }
constexpr int wbn(int s) {
  int n = (s ? nops(s - 2) - 6 : 0) + nops(s - 1);
  const int u = (s + 1) / 2;
  if ((s & 1) && u < 8) {
    int m = (XG && u > 1 && !(u & 1) ? 4 : 0) + pvn(xstep(u));
    for (int t = xstep(u) + 1; t < s; ++t) m += nops(t);
    n = m < n ? m : n;
  }
  return n;
}
constexpr int AUX_SC1 = AUXST;
#define AUXLD 16
#define PDEPTH 8

#define CODE_LINES 487
template <int I> DEV void* karg() { void* p; asm volatile("s_mov_b64 %0, s[%1:%2]" : "=s"(p) : "i"(I < 10 ? 80 + 2 * I : 78), "i"(I < 10 ? 81 + 2 * I : 79)); return p; }
#define SPF 1
#define NRND 2
#define RND0 4
#define RNDS 2
#define NPI 12
#define NPA 6
#define SPM 1
#define SPH 0
constexpr int IPR = SPH ? 4 : 8;
constexpr int PR = (SPM ? 8 : 4) * IPR;
template <int R> DEV void spf(const char* base, unsigned koff, int q0, int q1) {
#pragma unroll
  for (int q = q0; q < q1; ++q)
    if (SPF && q < PR * NRND && !(SPM && R == 86)) {
      const unsigned so = ((4 * (RND0 + RNDS * (q / PR)) + koff) & 31) * 128;
      asm volatile("s_load_dword s[%3], %0, %1 offset:%2" ::"s"(base), "s"(so), "i"(((q % PR) / IPR) * 4096 + (q % IPR) * (512 / IPR)), "n"(R) : "memory");
    }
}
#define ARGWAIT() asm volatile("s_waitcnt lgkmcnt(0)" ::: "memory"); \
  char* const ws = (char*)karg<10>(); const rsrc_t rW = mkrs(ws, WS_USED)

__global__ __launch_bounds__(512, 2) __attribute__((amdgpu_num_sgpr(84))) void fused_kernel(const u16*, const u16*, const u16*, const u16*, const u16*, const u16*,
                                                       const u16*, const u16*, const u16*, u16*, char*) {
  extern __shared__ __attribute__((aligned(16))) char smem[];
  asm volatile("s_load_dwordx16 s[80:95], %0, 0x0\n\ts_load_dwordx4 s[96:99], %0, 0x40\n\ts_load_dwordx2 s[78:79], %0, 0x50"
               :: "s"(__builtin_amdgcn_kernarg_segment_ptr())
               : "s80", "s81", "s82", "s83", "s84", "s85", "s86", "s87", "s88", "s89", "s90", "s91", "s92", "s93", "s94", "s95", "s96", "s97", "s98", "s99", "s78", "s79");
  const int tid = threadIdx.x, lane = tid & 63;
  const int b = blockIdx.x, xcd = b & 7, idx = b >> 3;
  u64 pc;
  {
    asm volatile("s_getpc_b64 %0" : "=s"(pc));
    const unsigned line = min((unsigned)((idx & 7) * 64 + (tid >> 3)), (unsigned)CODE_LINES - 1);
    asm volatile("global_load_dword v255, %0, %1" :: "v"(line * 128), "s"(pc) : "memory", "v255");
  }
  const int w = __builtin_amdgcn_readfirstlane(tid >> 6);
  const int ii = idx & 15;
  const int par = ii >> 3, rb = xcd * 2 + par, j = ii & 7;
  const unsigned ldsb = (unsigned)(size_t)(LAS char*)smem;
  const unsigned koff = (KX * xcd) & 31;
  const unsigned fme = WS_FLAG + (rb * 8 + j) * 1024;
  const int li = lane & 15, lg = lane >> 4, lq = li >> 2, lp = lane & 3;

  const unsigned yrot = ROTX * ((7 * par + 2 * xcd) % 14);
  auto yblk = [&](unsigned p) { const unsigned r = p - 2 + yrot; return (2 * j + (p < 2 ? p : 2 + (r >= 14 ? r - 14 : r))) & 15; };
  if (idx < 16) {
    const int ri = j >> 2, dt = j & 3;
    if (w >= 4) {
      const int lw = w - 4;
      const int r8 = lane >> 3, p8 = lane & 7;
      unsigned vC[4], vX[4];
#pragma unroll
      for (int q = 0; q < 4; ++q) {
        vC[q] = (8 * q + r8) * 4096 + ((p8 ^ (4 * (q & 1) + lg)) << 4) - q * 1024;
        vX[q] = (3 * q + lg) * 1024 + ((li ^ ((lg | (4 * (q >> 1))) << 1)) << 4);
      }
      const unsigned vD = r8 * 1024 + ((p8 ^ (((r8 >> 1) & 1) | (lw << 1))) << 4);
      const unsigned sC = (rb * 128 + 32 * lw) * 4096, sX = (16 * lw) * 1024 + dt * 256, sD = (j * 64 + 8 * lw) * 1024;
      const unsigned lat = dt + (ii >> 2);
      const unsigned mb = ldsb + lw * 4096;
#pragma unroll
      for (int q = 0; q < 4; ++q) { pin(vC[q]); pin(vX[q]); }
      unsigned oC, oX;
      auto prep = [&](int s) {
        unsigned ko = koff, la = lat;
        pins(ko); pins(la);
        const unsigned k = s >= PAIR ? (((s & ~7) + ko) & 31) + 2 * (((s >> 1) + la) & 3) + (s & 1) : (((s & ~3) + ko) & 31) + ((s + la) & 3);
        oC = sC + k * 128; oX = sX + k * 65536;
        pins(oC); pins(oX);
      };
      prep(0);
      ARGWAIT();
      void* const pCr = karg<1>(); void* const pCi = karg<2>();
      const rsrc_t rC = mkrs(ri ? pCi : pCr, S * S * 2), rX = mkrs(karg<0>(), S * D * 2);
      auto issue = [&](int s) {
        const bool xf = s >= PAIR && (s & 7) == 0;
        if (xf) dma4<0>(rX, vX, oX, mb + (s % 5) * STB + 16384);
        dma4<CNT>(rC, vC, oC, mb + (s % 5) * STB);
        if (!xf) dma4<0>(rX, vX, oX, mb + (s % 5) * STB + 16384);
      };
      auto issueD = [&](int grp, unsigned slot) {
        const rsrc_t rDr = mkrs(karg<3>(), D * D * 2), rDi = mkrs(karg<4>(), D * D * 2);
#pragma unroll
        for (int q = 0; q < 4; ++q) {
          unsigned pos = 4 * grp + q;
          if (pos >= 2) pins(pos);
          const unsigned blk = yblk(pos);
          const rsrc_t r = blk < 8 ? rDr : rDi;
          dma16<0>(r, vD, sD + (blk & 7) * 128, slot + q * 8192 + lw * 1024);
          dma16<0>(r, vD, sD + 32768 + (blk & 7) * 128, slot + q * 8192 + lw * 1024 + 4096);
        }
      };
      u64 tagm = 0, pa = 0, pb = 0;
      if (APF) {
        const unsigned ub = (ii + xcd) & 7, fb = ((xcd < 7 ? 176 * xcd : 1200) * 2) & ~63u;
        const char* const pw = (const char*)(lw & 1 ? karg<6>() : karg<5>()) + ((2 * ub + par) * 32 + 16 * (lw >> 1)) * (F * 2) + fb;
        const unsigned vw = (APF & 4 ? lane >> 2 : 8 + ((lane >> 2) & 7)) * (F * 2) + min((lane & 3) * 128, 380);
        asm volatile("global_load_dword v255, %0, %1" ::"v"(vw), "s"(pw) : "memory", "v255");
        if (APF & 1) {
          const char* const px = (const char*)karg<0>() + (ii * 128 + 32 * lw) * 1024;
          asm volatile("global_load_dword v255, %0, %1" ::"v"((lane & 31) * 1024 + ((ub + 1 + (lane >> 5)) & 7) * 128), "s"(px) : "memory", "v255");
        }
      }
      issue(0); prep(1); issue(1);
      ld64a(tagm, rW, 0, WS_SALT + (rb * 8 + j) * 8);
      prep(2);
      unsigned pfr = 256 * xcd + ((idx >> 1) * 4 + lw) * (PR / IPR);
      pins(pfr);
      const char* const pfb = (const char*)((idx & 1) ? pCi : pCr) + (size_t)pfr * 4096;
      spf<90>(pfb, koff, 0, NPI);
      waitvm<9>(); bar();
      issue(2);
      if (LA == 4) { prep(3); issue(3); }
#pragma unroll
      for (int s = 0; s < 32 - LA; ++s) {
        prep(s + LA);
        waitvm<8 * (LA - 2)>(); bar();
        issue(s + LA);
        spf<90>(pfb, koff, NPI + NPA * s, NPI + NPA * (s + 1));
      }
      if (LA == 4) {
        waitvm<16>(tagm); bar();
        issueD(0, ldsb + 2 * STB);
        waitvm<16>(); bar();
      } else {
        waitvm<8>(tagm); bar();
        issueD(0, ldsb + 2 * STB);
      }
      waitvm<8>(); bar();
      if (D1POS == 0) issueD(1, ldsb + 3 * STB);
      if (D2POS == 0) issueD(2, ldsb + 4 * STB);
      tagm = next_tag(tagm);
      waitvm<8 * ((D1POS == 0) + (D2POS == 0))>();
      bar();
      if (D1POS == 1 || D2POS == 1 || D3POS == 1) __builtin_amdgcn_s_sleep(2);
      if (D1POS == 1) issueD(1, ldsb + 3 * STB);
      if (D2POS == 1) issueD(2, ldsb + 4 * STB);
      if (D3POS == 1) issueD(3, ldsb);
      if (lw == 0) { poll(pa, pb, rW, (lane & 31) * 8 + (lane >> 5) * 512, fme, tagm); waitvm<1>(); } else waitvm<0>();
      bar();
      if (D2POS == 2 || D3POS == 2) __builtin_amdgcn_s_sleep(D2SL);
      if (D2POS == 2) issueD(2, ldsb + 4 * STB);
      auto issueG = [&]() {
        setm0(ldsb + STB + 16384 + lw * 256);
        asm volatile("buffer_load_dword %0, %1, %2 offen sc1 lds" ::"v"((lane & 31) * 4), "s"(rW), "s"(WS_G + rb * 512 + lw * 128) : "memory");
      };
      if (GPOS == 0) issueG();
      if (D3POS == 2) issueD(3, ldsb);
      if (GPOS == 1) issueG();
      if (D3POS == 3) { bar(); issueD(3, ldsb); }
      if (D3POS >= 2) { waitvm<0>(); bar(); }
      asm volatile("" : "+v"(pa), "+v"(pb));
      if (FFP) __builtin_amdgcn_s_sleep(FFP);
      poll(pa, pb, rW, 768 + lw * 8, fme, tagm);
      setm0(ldsb + STB + lw * 4096);
#define FFQ(q) asm volatile("buffer_load_dwordx4 %0, %1, %2 offen offset:" #q " sc1 lds" ::"v"(lane * 16), "s"(rW), "s"(WS_FF + rb * FST + j * 16384 + lw * 4096) : "memory")
      FFQ(0); FFQ(1024); FFQ(2048); FFQ(3072);
      if (GPOS == 2) issueG();
      asm volatile("s_waitcnt vmcnt(0)" : "+v"(pa), "+v"(pb) :: "memory");
      bar();
      if (lw == 0 && lane == 0) st64a(tagm, rW, 0, WS_SALT + (rb * 8 + j) * 8);
      return;
    }
    const int wd = w >> 1, wt = w & 1;
    unsigned aC[3][2], aX[3][4];
#pragma unroll
    for (int h = 0; h < 3; ++h) {
#pragma unroll
      for (int ks = 0; ks < 2; ++ks) { aC[h][ks] = ldsb + h * 65536 + (wt * 64 + li) * 128 + (((4 * ks + lg) ^ (li >> 1)) << 4); pin(aC[h][ks]); }
#pragma unroll
      for (int m = 0; m < 4; ++m) {
        aX[h][m] = ldsb + h * 65536 + 16384 + (8 * lg + lq) * 256 + (((2 * (wd * 4 + m) + (lp >> 1)) ^ ((lq | ((lg & 1) << 2)) << 1)) << 4) + 8 * (lp & 1);
        pin(aX[h][m]);
      }
    }
    unsigned vP[2];
#pragma unroll
    for (int M = 0; M < 2; ++M) { vP[M] = lane * 16 + WS_PQ + rb * PST + ((4 * j + 2 * wd + M) * 8 + 4 * wt) * 1024; pin(vP[M]); }
    ARGWAIT();
    u64 tagc = 0;
    ld64a(tagc, rW, (lane & 7) * 8, WS_SALT + rb * 64);
    unsigned aD[3][4], sP[32];
    auto setupY = [&]() {
#pragma unroll
      for (int h = 0; h < 3; ++h)
#pragma unroll
        for (int v = 0; v < 4; ++v) { aD[h][v] = ldsb + h * 65536 + (8 * lq + lp) * 128 + ((((lg >> 1) + 2 * v) ^ (li >> 1)) << 4) + 8 * (lg & 1); pin(aD[h][v]); }
#pragma unroll
      for (int kb = 0; kb < 32; ++kb) if (!(HOIST & 4)) { sP[kb] = WS_PQ + rb * PST + (2 * yblk(kb >> 1) + (kb & 1)) * 8192; pins(sP[kb]); }
    };
    unsigned pc0 = (2 * j + 2) * 16384, pcur = pc0 + yrot * 16384, pbase = WS_PQ + rb * PST;
    if (HOIST & 4) { pins(pc0); pins(pcur); pins(pbase); }
    auto stepP = [&](int p, int h) {
      if (h == 0) { sP[2 * p] = pbase + ((p < 2 ? pc0 + (p - 2) * 16384 : pcur) & 0x3c000); pins(sP[2 * p]); }
      if (h == 1) { sP[2 * p + 1] = sP[2 * p] + 8192; pins(sP[2 * p + 1]); }
      if (h == 2 && p >= 2 && p < 15) { pcur += 16384; pcur = pcur == pc0 + 14 * 16384 ? pc0 : pcur; pins(pcur); }
    };
    if (HOIST & 1) setupY();
    f32x4 acc[4][4];
    s16x4 xa[2][4][2];
    bf16x8 cb[2][4];
    auto rdS = [&](int r, int buf, unsigned so, int ks) {
      constexpr int kind[12] = {0, 1, 8, 9, 2, 3, 10, 11, 4, 5, 6, 7};
      const int k = kind[r];
      if (k < 8) xa[buf][k >> 1][k & 1] = ldtr(aX[so >> 16][k >> 1] + (so & 65535) + ks * 8192 + (k & 1) * 1024);
      else cb[buf][k - 8] = ld128(aC[so >> 16][ks] + (so & 65535) + (k - 8) * 2048);
    };
    bar();
#pragma unroll
    for (int r = 0; r < 12; ++r) rdS(r, 0, 0, 0);
#pragma unroll
    for (int s = 0; s < 32; ++s) {
      const unsigned so = (s % 5) * STB, sn = ((s + 1) % 5) * STB;
#pragma unroll
      for (int i = 0; i < 16; ++i) {
        if (s == 0) mfma0(acc[i >> 2][i & 3], join(xa[0][i >> 2][0], xa[0][i >> 2][1]), cb[0][i & 3]);
        else mfma(acc[i >> 2][i & 3], join(xa[0][i >> 2][0], xa[0][i >> 2][1]), cb[0][i & 3]);
        if (i < 12) rdS(i, 1, so, 1);
        else if ((HOIST & 4) && s >= SP0 && s < SP0 + 16) stepP(s - SP0, i - 12);
      }
      if (s < 31) bar();
#pragma unroll
      for (int i = 0; i < 16; ++i) {
        mfma(acc[i >> 2][i & 3], join(xa[1][i >> 2][0], xa[1][i >> 2][1]), cb[1][i & 3]);
        if (i < 12 && s < 31) rdS(i, 0, sn, 0);
      }
    }
    mfma_drain();
#pragma unroll
    for (int i = 0; i < 16; ++i) hold(acc[i >> 2][i & 3]);
    waitvm<0>(tagc);
    {
      auto publish = [&](bool ng) {
        u32x4 pv = __builtin_bit_cast(u32x4, acc[3][3]);
#pragma unroll
        for (int M = 0; M < 2; ++M)
#pragma unroll
          for (int n = 0; n < 4; ++n) {
            const f32x4 p = acc[2 * M][n], q = acc[2 * M + 1][n];
            u32x4 v = ng ? u32x4{pkneg(p[0], p[1]), pkneg(p[2], p[3]), pkneg(q[0], q[1]), pkneg(q[2], q[3])}
                               : u32x4{pk(p[0], p[1]), pk(p[2], p[3]), pk(q[0], q[1]), pk(q[2], q[3])};
            asm volatile("" : "+v"(v) : "v"(pv) : "memory");
            st128<AUX_SC1>(v, rW, vP[M] + n * 1024);
            pv = v;
          }
      };
      if (ri) publish(true); else publish(false);
    }
    if (!(HOIST & 1)) setupY();
    tagc = next_tag(tagc);
    asm volatile("" : "+v"(tagc));
    waitvm<0>();
    if (lane < 8) st64a(tagc, rW, lane * 1024, WS_FLAG + rb * 8192 + (j * 4 + w) * 8);
    bar();
    {
      f32x4 y[4][2];
      constexpr int PD = PDEPTH;
      u32x4 pa[PD + 1][2];
      s16x4 da[2][4][2];
      const unsigned vo = lane * 16 + w * 2048;
      auto ldP = [&](int kb, int n) { pa[kb % (PD + 1)][n] = __builtin_amdgcn_raw_buffer_load_b128(rW, vo + n * 1024, sP[kb], AUXLD); };
      auto rdD = [&](int kb, int r) {
        const unsigned base = (65536 + (kb >> 1) * 8192) % LDS_BYTES;
        const int mt = r >> 1, hl = r & 1;
        da[kb & 1][mt][hl] = ld64(aD[base >> 16][(kb & 1) * 2 + hl] + (base & 65535) + (mt >> 1) * 4096 + (mt & 1) * 512);
      };
#pragma unroll
      for (int kb = 0; kb < 4; ++kb) { ldP(kb, 0); ldP(kb, 1); }
#pragma unroll
      for (int r = 0; r < 8; ++r) { rdD(0, r); sep(); }
      unsigned aF = ldsb + STB + w * 4096 + lane * 16, aG = ldsb + STB + 16384 + (64 * w + li) * 4, vO[2];
#pragma unroll
      for (int n = 0; n < 2; ++n) { vO[n] = (rb * 128 + 32 * w + 16 * n + li) * 1024 + j * 128 + lg * 16; pin(vO[n]); }
      pin(aF); pin(aG);
      const rsrc_t rO = mkrs(karg<9>(), S * D * 2);
      u32x4 ff[4];
      float gg[2], og[2];
      bar();
#pragma unroll
      for (int kb = 4; kb < PD; ++kb) { ldP(kb, 0); ldP(kb, 1); }
      auto step = [&](int kb, int i) {
        const bf16x8 a = join(da[kb & 1][i >> 1][0], da[kb & 1][i >> 1][1]);
        const bf16x8 bb = __builtin_bit_cast(bf16x8, pa[kb % (PD + 1)][i & 1]);
        if (kb == 0) mfma0(y[i >> 1][i & 1], a, bb); else mfma(y[i >> 1][i & 1], a, bb);
        if (kb < 31) rdD(kb + 1, i);
        if (kb + PD < 32 && (i == 2 || i == 4)) ldP(kb + PD, i >> 2);
      };
#pragma unroll
      for (int kb = 0; kb < (LATEW ? 32 : WKB); ++kb) {
        if (LATEW && kb == 20) { gg[0] = *(const LAS float*)(size_t)aG; gg[1] = *(const LAS float*)(size_t)(aG + 64); }
        if (LATEW && kb == 24) {
#pragma unroll
          for (int n = 0; n < 2; ++n) asm volatile("v_sub_f32 %0, 1.0, %1" : "=v"(og[n]) : "v"(gg[n]));
        }
        if ((D3POS == 3 && kb == D3KB) || (D3POS >= 2 && kb == (D2POS >= 2 ? 15 : DWKB))) bar();
#pragma unroll
        for (int i = 0; i < 8; ++i) step(kb, i);
      }
      if (LATEW) {
        unsigned aT[2], aR = ldsb + 2 * STB + w * 4096 + (lane >> 3) * 128 + (((lane ^ (lane >> 3)) & 7) << 4), vT[4];
        u32x4 ro[4];
        if (OTR) {
#pragma unroll
          for (int M = 0; M < 2; ++M) { aT[M] = ldsb + 2 * STB + w * 4096 + li * 128 + (((4 * M + lg) ^ (li & 7)) << 4); pin(aT[M]); }
#pragma unroll
          for (int i = 0; i < 4; ++i) { vT[i] = (rb * 128 + 32 * w + 8 * i + (lane >> 3)) * 1024 + j * 128 + (lane & 7) * 16; pin(vT[i]); }
          pin(aR);
        }
        mfma_drain();
#pragma unroll
        for (int i = 0; i < 8; ++i) { hold(y[i >> 1][i & 1]); y[i >> 1][i & 1] *= gg[i & 1]; }
#pragma unroll
        for (int i = 0; i < 8; ++i) hold(y[i >> 1][i & 1]);
        bar();
#pragma unroll
        for (int q = 0; q < 4; ++q) { ff[q] = *(const LAS u32x4*)(size_t)(aF + q * 1024); sep(); }
        u32x4 pv = ff[3];
#pragma unroll
        for (int q = 0; q < 4; ++q) {
          const int n = q >> 1, M = q & 1;
          u32x4 v;
#pragma unroll
          for (int c = 0; c < 4; ++c) {
            const f32x4 p = y[2 * M + (c >> 1)][n];
            const unsigned f = ff[q][c];
            const f32x2 r = __builtin_elementwise_fma(f32x2{bflo(f), bfhi(f)}, f32x2{og[n], og[n]}, f32x2{p[2 * (c & 1)], p[2 * (c & 1) + 1]});
            v[c] = pk(r[0], r[1]);
          }
          if (OTR) *(LAS u32x4*)(size_t)(aT[M] + n * 2048) = v;
          else {
            asm volatile("" : "+v"(v) : "v"(pv) : "memory");
            st128<OAUX>(v, rO, vO[n] + M * 64);
            pv = v;
          }
          if (OTR && M) {
            const u32x4 r0 = *(const LAS u32x4*)(size_t)(aR + n * 2048), r1 = *(const LAS u32x4*)(size_t)(aR + n * 2048 + 1024);
            ro[2 * n] = r0; ro[2 * n + 1] = r1;
          }
        }
        if (OTR) {
#pragma unroll
          for (int i = 0; i < 4; ++i) st128<OAUX>(ro[i], rO, vT[i]);
        }
        return;
      }
    }
    return;
  }

  const int rbU = ii, sl = xcd;
  const int f0 = sl < 7 ? 176 * sl : 1200;
  const unsigned fbase = (f0 * 2) & ~63u;
  const unsigned u0 = (ii + xcd) & 7;
  auto unit = [&](unsigned u) { return (u + u0) & 7; };
  const int rot = ((HPF ? 0 : 5 * j) + 21 * par + ROTX * ((xcd * 11) >> 2)) % 43;
  auto hblk = [&](int bp) { const int kb = (HPF && bp < 32 ? (bp + 4 * j) & 31 : bp) + rot; return kb >= 43 ? kb - 43 : kb; };
  if (w >= 4) {
    const int lw = w - 4, mat = lw & 1;
    unsigned vW[6];
#pragma unroll
    for (int q = 0; q < 6; ++q) {
      const int L = 64 * q + lane, rl = L / 24, p = L - rl * 24;
      const int sw = 2 * (((rl >> 1) & 1) | (((rl >> 3) & 1) << 1)) + mat;
      vW[q] = rl * (F * 2) + ((p ^ sw) << 4) - (q & 3) * 1024;
    }
    const int r8 = lane >> 3, p8 = lane & 7;
    unsigned vX[4];
#pragma unroll
    for (int q = 0; q < 4; ++q) vX[q] = (7 * q + r8) * 1024 + ((p8 ^ (4 * (q & 1) + lg)) << 4);
    const unsigned sW = (16 * (lw >> 1)) * (F * 2) + fbase, sX = (rbU * 128 + 32 * lw) * 1024;
    const unsigned mW = ldsb + mat * 12288 + (lw >> 1) * 6144, mX = ldsb + XB + lw * 4096;
#pragma unroll
    for (int q = 0; q < 6; ++q) pin(vW[q]);
#pragma unroll
    for (int q = 0; q < 4; ++q) pin(vX[q]);
    ARGWAIT();
    void* const pWg = karg<5>(); void* const pWu = karg<6>();
    const rsrc_t rG = mkrs(mat ? pWu : pWg, D * F * 2), rX = mkrs(karg<0>(), S * D * 2);
    auto issW = [&](int s) {
      const unsigned so = sW + (2 * unit(s >> 1) + ((s & 1) ^ par)) * 32 * (F * 2);
      dma4<0>(rG, vW, so, mW + (s & 3) * WSL);
      setm0(mW + (s & 3) * WSL + 4096); dmaq<0, 0>(rG, vW[4], so); dmaq<1, 0>(rG, vW[5], so);
    };
    auto issX = [&](int u) { dma4<0>(rX, vX, sX + unit(u) * 128, mX + (u & 3) * 16384); };
    auto issXh = [&](int u, int h) {
      setm0(mX + (u & 3) * 16384);
      if (h) { dmaq<2, 0>(rX, vX[2], sX + unit(u) * 128); dmaq<3, 0>(rX, vX[3], sX + unit(u) * 128); }
      else { dmaq<0, 0>(rX, vX[0], sX + unit(u) * 128); dmaq<1, 0>(rX, vX[1], sX + unit(u) * 128); }
    };
    issX(0); issW(0);
    u32x4 gwv[4];
    u64 tagc = 0, tagm = 0, pa = 0, pb = 0;
    {
      const rsrc_t rGw = mkrs(karg<8>(), D * 2);
#pragma unroll
      for (int c = 0; c < 4; ++c)
        asm volatile("buffer_load_dwordx4 %0, %1, %2, 0 offen" : "=v"(gwv[c]) : "v"(unit(2 * c + (lane >> 5)) * 128 + p8 * 16), "s"(rGw) : "memory");
    }
    ld64a(tagc, rW, p8 * 8, WS_SALT + rbU * 64);
    ld64a(tagm, rW, 0, WS_SALT + (rb * 8 + j) * 8);
    issW(1);
    const rsrc_t rWd = mkrs(karg<7>(), F * D * 2);
    const int q4 = (lane >> 3) & 3, hf = lane >> 5;
    const unsigned vd0 = q4 * 1024 + (((lane & 7) ^ (((q4 >> 1) | ((lw & 1) << 1)) << 1)) << 4);
    const unsigned hk = hf * 1024;
    unsigned tabRow, tabDel;
    auto mktab = [&]() {
      unsigned ln = lane; pin(ln);
      const int kb = hblk(min((int)ln, 42));
      const int m = min((kb * 47) >> 9, 3), r = kb - 11 * m;
      const bool shared = r == 10 || kb == 42;
      const int s = 2 * m + (shared ? lw >> 1 : r >= 5), pos = r >= 5 ? r - 5 + (m == 3) : r;
      const int fs = s == 7 ? 1200 : 176 * s;
      const int row = shared ? 80 + 8 * (lw & 1) : pos == 4 ? 64 + 96 * (lw >> 1) + 4 * (lw & 1) : 96 * (pos >> 1) + 32 * (pos & 1) + 4 * lw;
      tabRow = (fs + row) * 1024 + j * 128; tabDel = shared ? 4 : pos == 4 ? 8 : 16;
      pin(tabRow); pin(tabDel);
    };
    auto issWd = [&](int b0, int b1) {
#pragma unroll
      for (int bp = b0; bp < b1; ++bp)
        dma16<2>(rWd, vd0 + hk * __builtin_amdgcn_readlane(tabDel, bp), __builtin_amdgcn_readlane(tabRow, bp), ldsb + wdpos(bp) + lw * 1024);
    };
    const int grow = 16 * sl + 4 * lw + (r8 & 3);
    const unsigned aG = ldsb + XB + (lane >> 5) * 16384 + grow * 128 + ((p8 ^ ((grow >> 1) & 7)) << 4);
    float gs = 0.f;
    u32x4 xv;
    auto gread = [&](int c) { xv = *(const LAS u32x4*)(size_t)(aG + (c & 1) * 32768); sep(); };
    auto gstep = [&](int c) {
      if (c < 3 && !GSPL) gread(c);
#pragma unroll
      for (int e = 0; e < 4; ++e) {
        const unsigned xe = xv[e], ge = gwv[c][e];
        gs = __builtin_amdgcn_fdot2_f32_bf16(__builtin_bit_cast(bf16x2, xe), __builtin_bit_cast(bf16x2, ge), gs, false);
      }
      asm volatile("" : "+v"(gs));
    };
    const char* const pCv = (const char*)(ii < 8 ? karg<1>() : karg<2>()) + (size_t)(256 * xcd + 32 * (ii & 7) + 8 * lw) * 4096;
    unsigned vPf = ((lane >> 2) & 7) * 4096 + (lane & 3) * 128;
    pin(vPf);
    auto pfC = [&](int s) {
#pragma unroll
      for (int q = 0; q < 6; ++q)
        if (pvs[q] == s) {
          const char* const src = pCv + ((4 * pvr[q] + koff) & 31) * 128;
          asm volatile("global_load_dword v255, %0, %1" ::"v"(vPf), "s"(src) : "memory", "v255");
        }
    };
    auto issXs = [&](int s) {
      if (!XG) issXh(2 + (s >> 1), s & 1);
      else if (xops(s)) { const int u = s == xs[0] ? 2 : s == xs[1] ? 4 : 6; issX(u); issX(u + 1); }
    };
#define WB(s, n) pfC(s - 1); waitvm<wbn(s)>(); bar()
    mktab();
    waitvm<12>(); bar();
    issW(2); issX(1);
    WB(0, 10); issW(3); issXs(0);
    WB(1, 8); issW(4); issXs(1);
    WB(2, 10); if (GSPL) gread(0); issW(5); issXs(2);
    asm volatile("" : "+v"(gwv[0]), "+v"(gwv[1]), "+v"(gwv[2]), "+v"(gwv[3]), "+v"(tagc), "+v"(tagm) :: "memory");
    gstep(0);
    WB(3, 8); issW(6); issXs(3);
    WB(4, 10); issW(7); issXs(4);
    WB(5, 8); issW(8); issXs(5);
    WB(6, 10); if (GSPL) gread(1); issW(9); issXs(6); gstep(1);
    WB(7, 8); issW(10); issXs(7);
    WB(8, 10); issW(11); issXs(8);
    WB(9, 8); issW(12); issXs(9);
    WB(10, 10); if (GSPL) gread(2); issW(13); issXs(10); gstep(2);
    WB(11, 8); issW(14); issXs(11);
    WB(12, 10); issW(15);
    tagc = next_tag(tagc); tagm = next_tag(tagm);
    asm volatile("" : "+v"(tagc), "+v"(tagm));
    WB(13, 6); issWd(0, WD1);
    WB(14, WD1);
    issWd(WD1, WD2);
    gread(3);
    asm volatile("" : "+v"(xv));
    bar();
    pfC(15);
    issWd(WD2, 40);
    gstep(3);
    gs += __int_as_float(__builtin_amdgcn_update_dpp(0, __float_as_int(gs), 0xb1, 15, 15, true));
    gs += __int_as_float(__builtin_amdgcn_update_dpp(0, __float_as_int(gs), 0x4e, 15, 15, true));
    gs += __int_as_float(__builtin_amdgcn_update_dpp(0, __float_as_int(gs), 0x141, 15, 15, true));
    {
      const auto sw = __builtin_amdgcn_permlane32_swap(__float_as_uint(gs), __float_as_uint(gs), false, false);
      gs = __uint_as_float(sw[0]) + __uint_as_float(sw[1]);
    }
    gs = bflo(pk(gs, gs));
    gs = __builtin_amdgcn_rcpf(1.f + __builtin_amdgcn_exp2f(-1.4426950408889634f * gs));
    gs = bflo(pk(gs, gs));
    asm volatile("" : "+v"(gs));
    auto stG = [&]() { if ((lane & 39) == 0) asm volatile("buffer_store_dword %0, %1, %2, %3 offen sc1" ::"v"(gs), "v"(r8 * 4), "s"(rW), "s"(WS_G + (rbU * 128 + 16 * sl + 4 * lw) * 4) : "memory"); };
    auto flG = [&]() { if (lane < 8) st64a(tagc, rW, lane * 1024, WS_FLAG + rbU * 8192 + 512 + (sl * 4 + lw) * 8); };
    if (GEARLY) { stG(); if (GEARLY == 1 || lw) { waitvm<0>(); flG(); } }
    if (lw == 0) { poll(pa, pb, rW, (lane & 31) * 8, fme + 256, tagm); waitvm<1>(); if (GEARLY == 2) flG(); }
    bar();
    bar();
    issWd(40, 43);
    if (!GEARLY) stG();
    asm volatile("s_waitcnt vmcnt(0)" : "+v"(pa), "+v"(pb) :: "memory");
    if (!GEARLY) flG();
    bar();
    asm volatile("s_waitcnt lgkmcnt(0)" ::: "memory");
    return;
  }
  const int wc = w >> 1, wt = w & 1;
  ARGWAIT();
  u64 tagc = 0;
  ld64a(tagc, rW, (lane < 8 ? rbU * 8 + lane : rb * 8 + j) * 8, WS_SALT);
  unsigned sH[43];
  auto setupH = [&]() {
#pragma unroll
    for (int bp = 0; bp < 43; ++bp) {
      sH[bp] = WS_H + rb * HST + hblk(bp) * 8192; pins(sH[bp]);
    }
  };
  if (HOIST & 2) setupH();
  f32x4 acc[11][4];
  {
    const int c0 = ((f0 * 2) & 63) >> 4;
    unsigned aW[11], aX[2];
    const int sw = 2 * ((lq >> 1) | ((lg & 1) << 1));
#pragma unroll
    for (int u = 0; u < 11; ++u) {
      const int mat = u < 10 ? u & 1 : lp >> 1, ch = u < 10 ? c0 + 12 * wc + (u & ~1) + (lp >> 1) : c0 + 10 + wc;
      aW[u] = ldsb + mat * 12288 + (8 * lg + lq) * 384 + ((ch ^ (sw + mat)) << 4) + 8 * (lp & 1);
      pin(aW[u]);
    }
#pragma unroll
    for (int ks = 0; ks < 2; ++ks) { aX[ks] = ldsb + XB + (64 * wt + li) * 128 + (((4 * (ks ^ par) + lg) ^ (li >> 1)) << 4); pin(aX[ks]); }
    s16x4 wf[3][2];
    bf16x8 xf[2][4];
    bar();
    wf[0][0] = ldtr(aW[0]); wf[0][1] = ldtr(aW[0] + 1536);
#pragma unroll
    for (int n = 0; n < 4; ++n) xf[0][n] = ld128(aX[0] + n * 2048);
    wf[1][0] = ldtr(aW[1]); wf[1][1] = ldtr(aW[1] + 1536);
#pragma unroll
    for (int s = 0; s < 16; ++s) {
      const int dl = (s & 3) == 1 ? 2 * (int)WSL : -2 * (int)WSL;
      const bool st = (s & 1) && s < 15;
      const unsigned so = (s & 1) * WSL, sn = ((s + 1) & 1) * WSL;
#pragma unroll
      for (int u = 0; u < 11; ++u) {
        const int cur = (s * 11 + u) % 3, nx = (s * 11 + u + 2) % 3, un = (u + 2) % 11;
        const bool more = s < 15 || u < 9;
        if (u == 9 && s < 15) bar();
#pragma unroll
        for (int n = 0; n < 4; ++n) {
          const bf16x8 a = join(wf[cur][0], wf[cur][1]);
          if (s == 0) mfma0(acc[u][n], a, xf[s & 1][n]); else mfma(acc[u][n], a, xf[s & 1][n]);
          if (u < 9) {
            if (n < 2) wf[nx][n] = ldtr(aW[un] + so + n * 1536);
            if (n == 2 && st) vadd(aW[u], dl);
          } else if (more) {
            if (n < 2) xf[(s + 1) & 1][(u - 9) * 2 + n] = ld128(aX[(s + 1) & 1] + (((s + 1) >> 1) & 3) * 16384 + ((u - 9) * 2 + n) * 2048);
            else wf[nx][n - 2] = ldtr(aW[un] + sn + (n - 2) * 1536);
            if (n == 1 && st) vadd(aW[u], dl);
          }
        }
      }
    }
  }
  mfma_drain();
#pragma unroll
  for (int i = 0; i < 44; ++i) hold(acc[i >> 2][i & 3]);
  waitvm<0>(tagc);
  bar();
  {
    const unsigned kb0 = sl < 7 ? 11 * (sl >> 1) + 5 * (sl & 1) : 37, kbs = sl < 6 ? 11 * (sl >> 1) + 10 : 42;
    const unsigned hb = WS_H + rbU * HST + wt * 4096;
    auto act2 = [&](float a0, float a1, float b0, float b1) {
      const f32x2 a = {a0, a1}, up = {b0, b1};
      const f32x2 k2 = {-0.1029432397f, -0.1029432397f}, k1 = {-2.302208198f, -2.302208198f}, one = {1.f, 1.f};
      const f32x2 z = a * __builtin_elementwise_fma(a * a, k2, k1);
      f32x2 e = {__builtin_amdgcn_exp2f(z[0]), __builtin_amdgcn_exp2f(z[1])};
      e += one;
      const f32x2 r = {__builtin_amdgcn_rcpf(e[0]), __builtin_amdgcn_rcpf(e[1])};
      const f32x2 h = (a * up) * r;
      return pk(h[0], h[1]);
    };
    auto act = [&](int p, int n, int e) { return act2(acc[2 * p][n][e], acc[2 * p][n][e + 1], acc[2 * p + 1][n][e], acc[2 * p + 1][n][e + 1]); };
    unsigned vH[2];
#pragma unroll
    for (int bq = 0; bq < 2; ++bq) { vH[bq] = lane * 16 + hb + (kb0 + 2 * wc + bq) * 8192; pin(vH[bq]); }
#pragma unroll
    for (int bq = 0; bq < 2; ++bq)
#pragma unroll
      for (int n = 0; n < 4; ++n) {
        const u32x4 v = {act(2 * bq, n, 0), act(2 * bq, n, 2), act(2 * bq + 1, n, 0), act(2 * bq + 1, n, 2)};
        if (bq || sl < 7 || wc) st128<AUX_SC1>(v, rW, vH[bq] + n * 1024);
      }
    unsigned v4 = (lane & 31) * 16 + (lane >> 5) * 1024 + hb + (kb0 + 4) * 8192 + wc * 512;
    pin(v4);
#pragma unroll
    for (int np = 0; np < 2; ++np) {
      const auto s0 = __builtin_amdgcn_permlane32_swap(act(4, 2 * np, 0), act(4, 2 * np + 1, 0), false, false);
      const auto s1 = __builtin_amdgcn_permlane32_swap(act(4, 2 * np, 2), act(4, 2 * np + 1, 2), false, false);
      const u32x4 v = {s0[0], s1[0], s0[1], s1[1]};
      st128<AUX_SC1>(v, rW, v4 + np * 2048);
    }
    {
      unsigned t[2][2];
#pragma unroll
      for (int np = 0; np < 2; ++np)
#pragma unroll
        for (int e = 0; e < 2; ++e) {
          const f32x4 p = acc[10][2 * np], q = acc[10][2 * np + 1];
          const auto s0 = __builtin_amdgcn_permlane32_swap(__float_as_uint(p[2 * e]), __float_as_uint(q[2 * e]), false, false);
          const auto s1 = __builtin_amdgcn_permlane32_swap(__float_as_uint(p[2 * e + 1]), __float_as_uint(q[2 * e + 1]), false, false);
          t[np][e] = act2(__uint_as_float(s0[0]), __uint_as_float(s1[0]), __uint_as_float(s0[1]), __uint_as_float(s1[1]));
        }
      const auto s0 = __builtin_amdgcn_permlane16_swap(t[0][0], t[1][0], false, false);
      const auto s1 = __builtin_amdgcn_permlane16_swap(t[0][1], t[1][1], false, false);
      const u32x4 v = {s0[0], s1[0], s0[1], s1[1]};
      st128<AUX_SC1>(v, rW, li * 16 + (2 * (lg & 1) + (lane >> 5)) * 1024 + hb + kbs * 8192 + (2 * (sl & 1) + wc) * 256);
    }
  }
  unsigned aV[3][4];
#pragma unroll
  for (int h = 0; h < 3; ++h)
#pragma unroll
    for (int m = 0; m < 4; ++m) {
      aV[h][m] = ldsb + h * 65536 + (8 * lg + lq) * 128 + (((2 * m + (lp >> 1)) ^ (((lq >> 1) | ((lg & 1) << 1)) << 1)) << 4) + 8 * (lp & 1);
      pin(aV[h][m]);
    }
  if (!(HOIST & 2)) setupH();
  tagc = next_tag(tagc);
  asm volatile("" : "+v"(tagc));
  waitvm<0>();
  if (lane < 8) st64a(tagc, rW, lane * 1024, WS_FLAG + rbU * 8192 + 256 + (sl * 4 + w) * 8);
  bar();
  {
    f32x4 y[4][2];
    constexpr int PD = PDEPTH;
    u32x4 pa[PD + 1][2];
    s16x4 da[2][4][2];
    const unsigned vo = lane * 16 + w * 2048;
    unsigned vF = lane * 16 + WS_FF + rb * FST + j * 16384 + w * 4096;
    pin(vF);
    auto ldH = [&](int bp, int n) { pa[bp % (PD + 1)][n] = __builtin_amdgcn_raw_buffer_load_b128(rW, vo + n * 1024, sH[bp], AUXLD); };
    auto rdV = [&](int bp, int r) {
      const unsigned base = wdpos(bp);
      da[bp & 1][r >> 1][r & 1] = ldtr(aV[base >> 16][r >> 1] + (base & 65535) + (r & 1) * 512);
    };
#pragma unroll
    for (int bp = 0; bp < PD / 2; ++bp) { ldH(bp, 0); ldH(bp, 1); }
#pragma unroll
    for (int r = 0; r < 8; ++r) { rdV(0, r); sep(); }
#pragma unroll
    for (int bp = PD / 2; bp < PD; ++bp) { ldH(bp, 0); ldH(bp, 1); }
#pragma unroll
    for (int bp = 0; bp < 43; ++bp) {
      if (bp == 3 || bp == 38) bar();
#pragma unroll
      for (int i = 0; i < 8; ++i) {
        const bf16x8 a = join(da[bp & 1][i >> 1][0], da[bp & 1][i >> 1][1]);
        const bf16x8 bb = __builtin_bit_cast(bf16x8, pa[bp % (PD + 1)][i & 1]);
        if (bp == 0) mfma0(y[i >> 1][i & 1], a, bb); else mfma(y[i >> 1][i & 1], a, bb);
        if (bp < 42) rdV(bp + 1, i);
        if (bp + PD < 43 && (i == 2 || i == 4)) ldH(bp + PD, i >> 2);
      }
    }
    mfma_drain();
#pragma unroll
    for (int i = 0; i < 8; ++i) hold(y[i >> 1][i & 1]);
#pragma unroll
    for (int n = 0; n < 2; ++n)
#pragma unroll
      for (int M = 0; M < 2; ++M) {
        const f32x4 p = y[2 * M][n], q = y[2 * M + 1][n];
        u32x4 v;
#pragma unroll
        for (int e = 0; e < 2; ++e) {
          const auto s1 = __builtin_amdgcn_permlane32_swap(pk(p[2 * e], p[2 * e + 1]), pk(q[2 * e], q[2 * e + 1]), false, false);
          const auto s2 = __builtin_amdgcn_permlane16_swap(s1[0], s1[1], false, false);
          v[e] = s2[0]; v[2 + e] = s2[1];
        }
        st128<AUX_SC1>(v, rW, vF + (n * 2 + M) * 1024);
      }
    waitvm<0>();
    u32x4 rr[4];
    auto reread = [&]() {
#pragma unroll
      for (int q = 0; q < 4; ++q)
        asm volatile("buffer_load_dwordx4 %0, %1, %2, 0 offen offset:%3 sc1" : "=v"(rr[q]) : "v"(vF), "s"(rW), "i"(q * 1024) : "memory");
    };
    if (RRD == 1) reread();
    if (lane == 8) st64a(tagc, rW, 0, fme + 768 + w * 8);
    if (RRD == 2) reread();
    if (RRD) {
      asm volatile("s_waitcnt vmcnt(0)" : "+v"(rr[0]), "+v"(rr[1]), "+v"(rr[2]), "+v"(rr[3]) :: "memory");
      ld64a(tagc, rW, 0, fme + 768);
      waitvm<0>(tagc);
    }
  }
}

extern "C" void kernel_launch(void* const* d_in, const int* in_sizes, int n_in, void* d_out, int out_size, void* d_ws, size_t ws_size,
                              hipStream_t stream) {
  (void)hipFuncSetAttribute((const void*)fused_kernel, hipFuncAttributeMaxDynamicSharedMemorySize, LDS_BYTES);
  fused_kernel<<<256, 512, LDS_BYTES, stream>>>((const u16*)d_in[0], (const u16*)d_in[1], (const u16*)d_in[2], (const u16*)d_in[3],
                                               (const u16*)d_in[4], (const u16*)d_in[6], (const u16*)d_in[7], (const u16*)d_in[8],
                                               (const u16*)d_in[9], (u16*)d_out, (char*)d_ws);
}
```
